```python
import torch

def _softmax(x, axis=-1):
    return torch.softmax(x, dim=axis)

def _gelu(x):
    return torch.nn.functional.gelu(x, approximate='tanh')

def _silu(x):
    return torch.nn.functional.silu(x)

def _sigmoid(x):
    return torch.sigmoid(x)

def _layernorm(x, axis=-1, eps=1e-05):
    m = x.mean(dim=axis, keepdim=True)
    v = x.var(dim=axis, keepdim=True, unbiased=False)
    return (x - m) / torch.sqrt(v + eps)

def _scatter_add(tgt, idx, src, axis=0):
    return tgt.clone().index_add_(axis, idx, src)

def _segment_sum(src, seg, n):
    out = torch.zeros((n,) + src.shape[1:], dtype=src.dtype)
    return out.index_add_(0, seg, src)

def _pad(x, pw):
    flat = tuple((v for pair in reversed(pw) for v in pair))
    return torch.nn.functional.pad(x, flat)
kernel_name = 'standalone__tree_speculative_verify_tree_attention_over_candidate_token_tree__sampling'

def setup_inputs():

    _w = lambda a: (lambda t: t.to(torch.bfloat16) if t.is_floating_point() else t)(torch.as_tensor(a))
    B, Hq, Hkv, T, C, D = (8, 32, 32, 16, 4096, 128)
    tree_mask = torch.zeros((B, T, T), dtype=float)
    for b in range(B):
        parent = torch.zeros(T, dtype=torch.int64)
        for i in range(1, T):
            parent[i] = int(torch.randint(0, i, (1,)).item())
        for i in range(T):
            j = i
            tree_mask[b, i, j] = 1.0
            while j > 0:
                j = parent[j]
                tree_mask[b, i, j] = 1.0
    return {'q': _w((lambda *s: torch.randn(*s, dtype=torch.bfloat16) * 0.8)(B, Hq, T, D)), 'k_cache': _w((lambda *s: torch.randn(*s, dtype=torch.bfloat16) * 0.8)(B, Hkv, C, D)), 'v_cache': _w((lambda *s: torch.randn(*s, dtype=torch.bfloat16) * 0.05)(B, Hkv, C, D)), 'k_tree': _w((lambda *s: torch.randn(*s, dtype=torch.bfloat16) * 0.8)(B, Hkv, T, D)), 'v_tree': _w((lambda *s: torch.randn(*s, dtype=torch.bfloat16) * 0.05)(B, Hkv, T, D)), 'tree_mask': _w(tree_mask)}

def reference(q, k_cache, v_cache, k_tree, v_tree, tree_mask):
    B, Hq, T, D = q.shape
    Hkv, C = (k_cache.shape[1], k_cache.shape[2])
    rep = Hq // Hkv
    kc = (lambda x, n, axis: torch.repeat_interleave(x, n, dim=axis))(k_cache, rep, axis=1)
    vc = (lambda x, n, axis: torch.repeat_interleave(x, n, dim=axis))(v_cache, rep, axis=1)
    kt = (lambda x, n, axis: torch.repeat_interleave(x, n, dim=axis))(k_tree, rep, axis=1)
    vt = (lambda x, n, axis: torch.repeat_interleave(x, n, dim=axis))(v_tree, rep, axis=1)
    k = torch.concatenate([kc, kt], axis=2)
    v = torch.concatenate([vc, vt], axis=2)
    scale = D ** (-0.5)
    scores = torch.einsum('bhqd,bhkd->bhqk', q, k) * scale
    ctx_mask = torch.ones((B, 1, T, C), dtype=scores.dtype)
    tm = tree_mask[:, None, :, :].clone()
    full_mask = torch.concatenate([ctx_mask, tm], axis=-1)
    scores = scores + (1.0 - full_mask) * -1000000000.0
    p = _softmax(scores, axis=-1)
    return torch.einsum('bhqk,bhkd->bhqd', p, v)
```

```cpp
#include <hip/hip_runtime.h>
#include <hip/hip_bf16.h>
#include <stdint.h>

#define NW 8
#define AUX 2
#define KPAT 1
#define ROT 0
#define INTERLEAVE 1

typedef unsigned u32x4 __attribute__((ext_vector_type(4)));
typedef unsigned u32x2 __attribute__((ext_vector_type(2)));
typedef float f32x4 __attribute__((ext_vector_type(4)));
typedef __bf16 bf16x8 __attribute__((ext_vector_type(8)));
typedef short s16x4 __attribute__((ext_vector_type(4)));

constexpr int Bn = 8, Hn = 32, Tn = 16, Cn = 4096, Dn = 128;
constexpr int NTILE = Cn / 32;
constexpr int TPW = NTILE / NW;
constexpr float SCALE = 0.08838834764831845f;
constexpr float LOG2E = 1.4426950408889634f;
constexpr float CS = SCALE * LOG2E;
constexpr int OSTR = 132;
constexpr int LDS_O = NW * 16 * OSTR * 4;
constexpr int LDS_ML = NW * 16 * 8;
constexpr int LDS_NEED = LDS_O + LDS_ML;
constexpr int LDS_BYTES = LDS_NEED > 84 * 1024 ? LDS_NEED : 84 * 1024;

__device__ __forceinline__ f32x4 mfma32(u32x4 a, u32x4 b, f32x4 c) {
  return __builtin_amdgcn_mfma_f32_16x16x32_bf16(__builtin_bit_cast(bf16x8, a), __builtin_bit_cast(bf16x8, b), c, 0, 0, 0);
}
__device__ __forceinline__ f32x4 mfma16(u32x2 a, u32x2 b, f32x4 c) {
  return __builtin_amdgcn_mfma_f32_16x16x16bf16_1k(__builtin_bit_cast(s16x4, a), __builtin_bit_cast(s16x4, b), c, 0, 0, 0);
}
__device__ __forceinline__ float xrow_max(float x) {
  auto s = __builtin_amdgcn_permlane16_swap(__float_as_uint(x), __float_as_uint(x), false, false);
  x = fmaxf(__uint_as_float(s[0]), __uint_as_float(s[1]));
  auto t = __builtin_amdgcn_permlane32_swap(__float_as_uint(x), __float_as_uint(x), false, false);
  return fmaxf(__uint_as_float(t[0]), __uint_as_float(t[1]));
}
__device__ __forceinline__ float xrow_sum(float x) {
  auto s = __builtin_amdgcn_permlane16_swap(__float_as_uint(x), __float_as_uint(x), false, false);
  x = __uint_as_float(s[0]) + __uint_as_float(s[1]);
  auto t = __builtin_amdgcn_permlane32_swap(__float_as_uint(x), __float_as_uint(x), false, false);
  return __uint_as_float(t[0]) + __uint_as_float(t[1]);
}
__device__ __forceinline__ unsigned cvtpk(float lo, float hi) {
  typedef float f32x2 __attribute__((ext_vector_type(2)));
  typedef __bf16 bf16x2 __attribute__((ext_vector_type(2)));
  f32x2 v = {lo, hi};
  return __builtin_bit_cast(unsigned, __builtin_convertvector(v, bf16x2));
}
__device__ __forceinline__ unsigned perm_lo(unsigned hi, unsigned lo) { return __builtin_amdgcn_perm(hi, lo, 0x05040100u); }
__device__ __forceinline__ unsigned perm_hi(unsigned hi, unsigned lo) { return __builtin_amdgcn_perm(hi, lo, 0x07060302u); }

#define SB() __builtin_amdgcn_sched_barrier(0)

struct KV { u32x4 k[8]; u32x4 v[8]; };

__global__ void __launch_bounds__(NW * 64, NW / 4)
tree_attn_kernel(const unsigned short* __restrict__ q, const unsigned short* __restrict__ kc, const unsigned short* __restrict__ vc,
                 const unsigned short* __restrict__ kt, const unsigned short* __restrict__ vt, const unsigned short* __restrict__ mask,
                 unsigned short* __restrict__ out) {
  extern __shared__ __attribute__((aligned(16))) unsigned char smem[];
  const int bh = blockIdx.x;
  const int tid = threadIdx.x, lane = tid & 63, w = __builtin_amdgcn_readfirstlane(tid >> 6);
  const int r = lane & 15, g = lane >> 4;

  const __amdgpu_buffer_rsrc_t rk = __builtin_amdgcn_make_buffer_rsrc((void*)(kc + (size_t)bh * Cn * Dn), 0, Cn * Dn * 2, 0x00020000);
  const __amdgpu_buffer_rsrc_t rv = __builtin_amdgcn_make_buffer_rsrc((void*)(vc + (size_t)bh * Cn * Dn), 0, Cn * Dn * 2, 0x00020000);

  const int koff = (4 * (r & 3) + (r >> 2)) * 256 + g * 16;
  const int voff = lane * 16;

  u32x4 qf[4];
  {
    const unsigned short* qp = q + ((size_t)bh * Tn + r) * Dn + g * 8;
#pragma unroll
    for (int s = 0; s < 4; ++s) qf[s] = *reinterpret_cast<const u32x4*>(qp + 32 * s);
  }

  auto tile_off = [&](int i) -> int {
    int t = i * NW + w;
    return t * 8192;
  };
#define LOADK(buf, so) do { _Pragma("unroll") for (int u = 0; u < 2; ++u) { _Pragma("unroll") for (int s = 0; s < 4; ++s) \
      buf[u * 4 + s] = __builtin_bit_cast(u32x4, __builtin_amdgcn_raw_buffer_load_b128(rk, koff + s * 64, (so) + u * 4096, AUX)); } } while (0)
#define LOADV(buf, so) do { _Pragma("unroll") for (int j = 0; j < 8; ++j) \
      buf[j] = __builtin_bit_cast(u32x4, __builtin_amdgcn_raw_buffer_load_b128(rv, voff + (j & 3) * 1024, (so) + (j >> 2) * 4096, AUX)); } while (0)

  u32x4 k0[8], v0[8], k1[8], v1[8];
  {
    const int o0 = tile_off(0), o1 = tile_off(1);
    LOADK(k0, o0); LOADV(v0, o0); LOADK(k1, o1); LOADV(v1, o1);
  }
  SB();

  float m = -INFINITY, l = 0.f;
  f32x4 acc[8];
#pragma unroll
  for (int n = 0; n < 8; ++n) acc[n] = f32x4{0.f, 0.f, 0.f, 0.f};

  if (w == 0) {
    u32x4 tk[4], tv[4];
    const unsigned short* kp = kt + ((size_t)bh * Tn + r) * Dn + g * 8;
    const unsigned short* vp = vt + ((size_t)bh * Tn + 4 * g) * Dn + r * 8;
#pragma unroll
    for (int s = 0; s < 4; ++s) tk[s] = *reinterpret_cast<const u32x4*>(kp + 32 * s);
#pragma unroll
    for (int j = 0; j < 4; ++j) tv[j] = *reinterpret_cast<const u32x4*>(vp + j * Dn);
    const u32x2 mk = *reinterpret_cast<const u32x2*>(mask + ((size_t)(bh / Hn) * Tn + r) * Tn + 4 * g);
    f32x4 s0 = {0.f, 0.f, 0.f, 0.f};
#pragma unroll
    for (int s = 0; s < 4; ++s) s0 = mfma32(tk[s], qf[s], s0);
    float mv[4] = {__uint_as_float(mk[0] << 16), __uint_as_float(mk[0] & 0xffff0000u), __uint_as_float(mk[1] << 16), __uint_as_float(mk[1] & 0xffff0000u)};
    float x[4];
#pragma unroll
    for (int i = 0; i < 4; ++i) x[i] = fmaf(s0[i], CS, (1.0f - mv[i]) * (-1.0e9f * LOG2E));
    float tm = xrow_max(fmaxf(fmaxf(x[0], x[1]), fmaxf(x[2], x[3])));
    m = tm;
    float p[4];
#pragma unroll
    for (int i = 0; i < 4; ++i) p[i] = __builtin_amdgcn_exp2f(x[i] - m);
    l = (p[0] + p[1]) + (p[2] + p[3]);
    const u32x2 pb = {cvtpk(p[0], p[1]), cvtpk(p[2], p[3])};
#pragma unroll
    for (int wd = 0; wd < 4; ++wd) {
      const u32x2 a0 = {perm_lo(tv[1][wd], tv[0][wd]), perm_lo(tv[3][wd], tv[2][wd])};
      const u32x2 a1 = {perm_hi(tv[1][wd], tv[0][wd]), perm_hi(tv[3][wd], tv[2][wd])};
      acc[2 * wd] = mfma16(a0, pb, acc[2 * wd]);
      acc[2 * wd + 1] = mfma16(a1, pb, acc[2 * wd + 1]);
    }
  }

#define STEP(kb, vb, PREF, nxt) do { \
    f32x4 s0 = {0.f, 0.f, 0.f, 0.f}, s1 = {0.f, 0.f, 0.f, 0.f}; \
    _Pragma("unroll") for (int s = 0; s < 4; ++s) { s0 = mfma32(kb[s], qf[s], s0); s1 = mfma32(kb[4 + s], qf[s], s1); } \
    SB(); \
    if (PREF) { LOADK(kb, nxt); } \
    SB(); \
    float tm = fmaxf(fmaxf(fmaxf(s0[0], s0[1]), fmaxf(s0[2], s0[3])), fmaxf(fmaxf(s1[0], s1[1]), fmaxf(s1[2], s1[3]))); \
    tm = xrow_max(tm) * CS; \
    const float mn = fmaxf(m, tm); \
    if (__any(mn > m)) { \
      const float alpha = __builtin_amdgcn_exp2f(m - mn); \
      l *= alpha; \
      _Pragma("unroll") for (int n = 0; n < 8; ++n) acc[n] *= alpha; \
      m = mn; \
    } \
    float p[8]; \
    _Pragma("unroll") for (int i = 0; i < 4; ++i) { p[i] = __builtin_amdgcn_exp2f(fmaf(s0[i], CS, -m)); p[4 + i] = __builtin_amdgcn_exp2f(fmaf(s1[i], CS, -m)); } \
    l += ((p[0] + p[1]) + (p[2] + p[3])) + ((p[4] + p[5]) + (p[6] + p[7])); \
    const u32x4 pb = {cvtpk(p[0], p[1]), cvtpk(p[2], p[3]), cvtpk(p[4], p[5]), cvtpk(p[6], p[7])}; \
    _Pragma("unroll") for (int wd = 0; wd < 4; ++wd) { \
      const u32x4 a0 = {perm_lo(vb[1][wd], vb[0][wd]), perm_lo(vb[3][wd], vb[2][wd]), perm_lo(vb[5][wd], vb[4][wd]), perm_lo(vb[7][wd], vb[6][wd])}; \
      const u32x4 a1 = {perm_hi(vb[1][wd], vb[0][wd]), perm_hi(vb[3][wd], vb[2][wd]), perm_hi(vb[5][wd], vb[4][wd]), perm_hi(vb[7][wd], vb[6][wd])}; \
      acc[2 * wd] = mfma32(a0, pb, acc[2 * wd]); \
      acc[2 * wd + 1] = mfma32(a1, pb, acc[2 * wd + 1]); \
    } \
    SB(); \
    if (PREF) { LOADV(vb, nxt); } \
    SB(); \
  } while (0)

#pragma unroll 1
  for (int i = 0; i < TPW - 2; i += 2) {
    const int n0 = tile_off(i + 2), n1 = tile_off(i + 3);
    STEP(k0, v0, true, n0);
    STEP(k1, v1, true, n1);
  }
  STEP(k0, v0, false, 0);
  STEP(k1, v1, false, 0);

  l = xrow_sum(l);
  float* so = reinterpret_cast<float*>(smem) + (w * 16 + r) * OSTR + 32 * g;
#pragma unroll
  for (int i = 0; i < 4; ++i) {
    *reinterpret_cast<f32x4*>(so + 8 * i) = f32x4{acc[0][i], acc[1][i], acc[2][i], acc[3][i]};
    *reinterpret_cast<f32x4*>(so + 8 * i + 4) = f32x4{acc[4][i], acc[5][i], acc[6][i], acc[7][i]};
  }
  float2* sml = reinterpret_cast<float2*>(smem + LDS_O);
  if (g == 0) sml[w * 16 + r] = make_float2(m, l);
  __syncthreads();
  for (int e = tid; e < 16 * 32; e += NW * 64) {
    const int qq = e >> 5, d4 = (e & 31) * 4;
    float mw[NW], lw[NW];
    float M = -INFINITY;
#pragma unroll
    for (int ww = 0; ww < NW; ++ww) { const float2 t = sml[ww * 16 + qq]; mw[ww] = t.x; lw[ww] = t.y; M = fmaxf(M, t.x); }
    float L = 0.f;
    f32x4 o = {0.f, 0.f, 0.f, 0.f};
#pragma unroll
    for (int ww = 0; ww < NW; ++ww) {
      const float wt = __builtin_amdgcn_exp2f(mw[ww] - M);
      L = fmaf(lw[ww], wt, L);
      const f32x4 t = *reinterpret_cast<const f32x4*>(reinterpret_cast<const float*>(smem) + (ww * 16 + qq) * OSTR + d4);
      o += t * wt;
    }
    const float inv = 1.0f / L;
    const u32x2 ob = {cvtpk(o[0] * inv, o[1] * inv), cvtpk(o[2] * inv, o[3] * inv)};
    *reinterpret_cast<u32x2*>(out + ((size_t)bh * Tn + qq) * Dn + d4) = ob;
  }
}

extern "C" void kernel_launch(void* const* d_in, const int* in_sizes, int n_in, void* d_out, int out_size, void* d_ws, size_t ws_size,
                              hipStream_t stream) {
  (void)in_sizes; (void)n_in; (void)out_size; (void)d_ws; (void)ws_size;
  hipFuncSetAttribute((const void*)tree_attn_kernel, hipFuncAttributeMaxDynamicSharedMemorySize, LDS_BYTES);
  tree_attn_kernel<<<dim3(Bn * Hn), dim3(NW * 64), LDS_BYTES, stream>>>(
      (const unsigned short*)d_in[0], (const unsigned short*)d_in[1], (const unsigned short*)d_in[2], (const unsigned short*)d_in[3],
      (const unsigned short*)d_in[4], (const unsigned short*)d_in[5], (unsigned short*)d_out);
}
```

```cpp
#include <hip/hip_runtime.h>
#include <stdint.h>

typedef unsigned u32x4 __attribute__((ext_vector_type(4)));
typedef unsigned u32x2 __attribute__((ext_vector_type(2)));
typedef float f32x4 __attribute__((ext_vector_type(4)));
typedef float f32x2 __attribute__((ext_vector_type(2)));
typedef __bf16 bf16x8 __attribute__((ext_vector_type(8)));
typedef __bf16 bf16x2 __attribute__((ext_vector_type(2)));
typedef short s16x4 __attribute__((ext_vector_type(4)));

constexpr int Bn = 8, Hn = 32, Tn = 16, Cn = 4096, Dn = 128;
constexpr int NW = 2;
constexpr int NTILE = Cn / 32;
constexpr int TPW = NTILE / NW;
constexpr float SCALE = 0.08838834764831845f;
constexpr float LOG2E = 1.4426950408889634f;
constexpr float CS = SCALE * LOG2E;
constexpr int LDS_BYTES = 84 * 1024;
constexpr int HEAD_BYTES = Cn * Dn * 2;
static_assert(TPW % 2 == 0, "shape");

__device__ __forceinline__ f32x4 mfma32(u32x4 a, u32x4 b, f32x4 c) {
  return __builtin_amdgcn_mfma_f32_16x16x32_bf16(__builtin_bit_cast(bf16x8, a), __builtin_bit_cast(bf16x8, b), c, 0, 0, 0);
}
__device__ __forceinline__ f32x4 mfma16(u32x2 a, u32x2 b, f32x4 c) {
  return __builtin_amdgcn_mfma_f32_16x16x16bf16_1k(__builtin_bit_cast(s16x4, a), __builtin_bit_cast(s16x4, b), c, 0, 0, 0);
}
__device__ __forceinline__ float xrow_max(float x) {
  auto s = __builtin_amdgcn_permlane16_swap(__float_as_uint(x), __float_as_uint(x), false, false);
  x = fmaxf(__uint_as_float(s[0]), __uint_as_float(s[1]));
  auto t = __builtin_amdgcn_permlane32_swap(__float_as_uint(x), __float_as_uint(x), false, false);
  return fmaxf(__uint_as_float(t[0]), __uint_as_float(t[1]));
}
__device__ __forceinline__ float xrow_sum(float x) {
  auto s = __builtin_amdgcn_permlane16_swap(__float_as_uint(x), __float_as_uint(x), false, false);
  x = __uint_as_float(s[0]) + __uint_as_float(s[1]);
  auto t = __builtin_amdgcn_permlane32_swap(__float_as_uint(x), __float_as_uint(x), false, false);
  return __uint_as_float(t[0]) + __uint_as_float(t[1]);
}
__device__ __forceinline__ unsigned cvtpk(float lo, float hi) {
  const f32x2 v = {lo, hi};
  return __builtin_bit_cast(unsigned, __builtin_convertvector(v, bf16x2));
}
__device__ __forceinline__ unsigned perm_lo(unsigned hi, unsigned lo) { return __builtin_amdgcn_perm(hi, lo, 0x05040100u); }
__device__ __forceinline__ unsigned perm_hi(unsigned hi, unsigned lo) { return __builtin_amdgcn_perm(hi, lo, 0x07060302u); }
__device__ __forceinline__ u32x4 mkrs(const void* p, size_t byte_off, unsigned bytes) {
  const uint64_t a = (uint64_t)p + byte_off;
  return u32x4{(unsigned)a, (unsigned)(a >> 32) & 0xffffu, bytes, 0x00020000u};
}
#define SB() __builtin_amdgcn_sched_barrier(0)

template <int IMM, bool FIRST> __device__ __forceinline__ void bload(u32x4& d, int voff, u32x4 rs, int soff) {
  if constexpr (FIRST) asm volatile("buffer_load_dwordx4 %0, %1, %2, %3 offen offset:%c4 nt" : "=v"(d) : "v"(voff), "s"(rs), "s"(soff), "i"(IMM) : "memory");
  else                 asm volatile("buffer_load_dwordx4 %0, %1, %2, %3 offen offset:%c4 nt" : "+v"(d) : "v"(voff), "s"(rs), "s"(soff), "i"(IMM) : "memory");
}
template <int N> __device__ __forceinline__ void wait_pair(u32x4 (&a)[4], u32x4 (&b)[4]) {
  asm volatile("s_waitcnt vmcnt(%c8)" : "+v"(a[0]), "+v"(a[1]), "+v"(a[2]), "+v"(a[3]), "+v"(b[0]), "+v"(b[1]), "+v"(b[2]), "+v"(b[3]) : "i"(N) : "memory");
}
template <int KIND, bool FIRST> __device__ __forceinline__ void issue(u32x4 (&d)[4], u32x4 rk, u32x4 rv, int koff, int voff, int so) {
  if constexpr (KIND < 2) {
    const int s = so + KIND * 4096;
    bload<0, FIRST>(d[0], koff, rk, s); bload<64, FIRST>(d[1], koff, rk, s); bload<128, FIRST>(d[2], koff, rk, s); bload<192, FIRST>(d[3], koff, rk, s);
  } else {
    const int s = so + (KIND - 2) * 4096;
    bload<0, FIRST>(d[0], voff, rv, s); bload<1024, FIRST>(d[1], voff, rv, s); bload<2048, FIRST>(d[2], voff, rv, s); bload<3072, FIRST>(d[3], voff, rv, s);
  }
}

struct State { float m, l; f32x4 acc[8]; f32x4 s0; u32x4 pb; };

template <int C> __device__ __forceinline__ void consume(u32x4 (&ring)[8][4], const u32x4 (&qf)[4], State& st, u32x4 rk, u32x4 rv, int koff, int voff, const int (&so)[3]) {
  if constexpr (C % 2 == 0) {
    wait_pair<8>(ring[C], ring[C + 1]);
    SB();
    issue<C % 4, false>(ring[(C + 4) % 8], rk, rv, koff, voff, so[C / 4 + 1]);
    issue<C % 4 + 1, false>(ring[(C + 5) % 8], rk, rv, koff, voff, so[C / 4 + 1]);
    SB();
  }
  if constexpr (C % 4 == 0) {
    f32x4 s = {0.f, 0.f, 0.f, 0.f};
#pragma unroll
    for (int k = 0; k < 4; ++k) s = mfma32(ring[C][k], qf[k], s);
    st.s0 = s;
  } else if constexpr (C % 4 == 1) {
    f32x4 s1 = {0.f, 0.f, 0.f, 0.f};
#pragma unroll
    for (int k = 0; k < 4; ++k) s1 = mfma32(ring[C][k], qf[k], s1);
    const f32x4 s0 = st.s0;
    float tm = fmaxf(fmaxf(fmaxf(s0[0], s0[1]), fmaxf(s0[2], s0[3])), fmaxf(fmaxf(s1[0], s1[1]), fmaxf(s1[2], s1[3])));
    tm = xrow_max(tm) * CS;
    const float mn = fmaxf(st.m, tm);
    const float alpha = __builtin_amdgcn_exp2f(st.m - mn);
    st.l *= alpha;
#pragma unroll
    for (int n = 0; n < 8; ++n) st.acc[n] *= alpha;
    st.m = mn;
    float p[8];
#pragma unroll
    for (int i = 0; i < 4; ++i) { p[i] = __builtin_amdgcn_exp2f(fmaf(s0[i], CS, -mn)); p[4 + i] = __builtin_amdgcn_exp2f(fmaf(s1[i], CS, -mn)); }
    st.l += ((p[0] + p[1]) + (p[2] + p[3])) + ((p[4] + p[5]) + (p[6] + p[7]));
    st.pb = u32x4{cvtpk(p[0], p[1]), cvtpk(p[2], p[3]), cvtpk(p[4], p[5]), cvtpk(p[6], p[7])};
  } else if constexpr (C % 4 == 3) {
    u32x4(&va)[4] = ring[C - 1];
    u32x4(&vb)[4] = ring[C];
#pragma unroll
    for (int wd = 0; wd < 4; ++wd) {
      const u32x4 a0 = {perm_lo(va[1][wd], va[0][wd]), perm_lo(va[3][wd], va[2][wd]), perm_lo(vb[1][wd], vb[0][wd]), perm_lo(vb[3][wd], vb[2][wd])};
      const u32x4 a1 = {perm_hi(va[1][wd], va[0][wd]), perm_hi(va[3][wd], va[2][wd]), perm_hi(vb[1][wd], vb[0][wd]), perm_hi(vb[3][wd], vb[2][wd])};
      st.acc[2 * wd] = mfma32(a0, st.pb, st.acc[2 * wd]);
      st.acc[2 * wd + 1] = mfma32(a1, st.pb, st.acc[2 * wd + 1]);
    }
  }
  SB();
}

template <int W> __device__ __forceinline__ void merge_send(const State& st, float lsum, unsigned char* smem, int lane) {
  f32x4* xo = reinterpret_cast<f32x4*>(smem);
  f32x2* xm = reinterpret_cast<f32x2*>(smem + 8192);
  constexpr int S = 2 * (1 - W);
#pragma unroll
  for (int j = 0; j < 2; ++j) {
    xo[(W * 4 + 2 * j) * 64 + lane] = f32x4{st.acc[0][S + j], st.acc[1][S + j], st.acc[2][S + j], st.acc[3][S + j]};
    xo[(W * 4 + 2 * j + 1) * 64 + lane] = f32x4{st.acc[4][S + j], st.acc[5][S + j], st.acc[6][S + j], st.acc[7][S + j]};
  }
  xm[W * 64 + lane] = f32x2{st.m, lsum};
}
template <int W> __device__ __forceinline__ void merge_recv(const State& st, float lsum, const unsigned char* smem, int lane, unsigned short* orow) {
  const f32x4* xo = reinterpret_cast<const f32x4*>(smem);
  const f32x2* xm = reinterpret_cast<const f32x2*>(smem + 8192);
  constexpr int K = 2 * W;
  const f32x2 o = xm[(1 - W) * 64 + lane];
  const float M = fmaxf(st.m, o[0]);
  float a = __builtin_amdgcn_exp2f(st.m - M), b = __builtin_amdgcn_exp2f(o[0] - M);
  const float inv = 1.0f / fmaf(lsum, a, o[1] * b);
  a *= inv; b *= inv;
#pragma unroll
  for (int j = 0; j < 2; ++j) {
    const f32x4 x0 = xo[((1 - W) * 4 + 2 * j) * 64 + lane], x1 = xo[((1 - W) * 4 + 2 * j + 1) * 64 + lane];
    const u32x4 ob = {cvtpk(fmaf(st.acc[0][K + j], a, x0[0] * b), fmaf(st.acc[1][K + j], a, x0[1] * b)),
                      cvtpk(fmaf(st.acc[2][K + j], a, x0[2] * b), fmaf(st.acc[3][K + j], a, x0[3] * b)),
                      cvtpk(fmaf(st.acc[4][K + j], a, x1[0] * b), fmaf(st.acc[5][K + j], a, x1[1] * b)),
                      cvtpk(fmaf(st.acc[6][K + j], a, x1[2] * b), fmaf(st.acc[7][K + j], a, x1[3] * b))};
    *reinterpret_cast<u32x4*>(orow + 8 * (K + j)) = ob;
  }
}

__global__ void __launch_bounds__(NW * 64, 2)
tree_attn_kernel(const unsigned short* __restrict__ q, const unsigned short* __restrict__ kc, const unsigned short* __restrict__ vc,
                 const unsigned short* __restrict__ kt, const unsigned short* __restrict__ vt, const unsigned short* __restrict__ mask,
                 unsigned short* __restrict__ out) {
  extern __shared__ __attribute__((aligned(16))) unsigned char smem[];
  asm volatile("" :: "s"(q), "s"(kc), "s"(vc), "s"(kt), "s"(vt), "s"(mask), "s"(out));
  const int bh = (blockIdx.x & 7) * 32 + (blockIdx.x >> 3);
  const int tid = threadIdx.x, lane = tid & 63, w = __builtin_amdgcn_readfirstlane(tid >> 6);
  const int r = lane & 15, g = lane >> 4;

  const int koff = (4 * (r & 3) + (r >> 2)) * 256 + g * 16;
  const int voff = lane * 16;
  const int qoff = r * 256 + g * 16;
  const u32x4 rk = mkrs(kc, (size_t)bh * HEAD_BYTES, HEAD_BYTES);
  const u32x4 rv = mkrs(vc, (size_t)bh * HEAD_BYTES, HEAD_BYTES);
  const u32x4 rq = mkrs(q, (size_t)bh * (Tn * Dn * 2), Tn * Dn * 2);
  const int tstep = NW * 8192;
  const int rot = (bh & 1) * (HEAD_BYTES / 2);
  auto toff = [rot](int x) -> int { return x < HEAD_BYTES ? ((x + rot) & (HEAD_BYTES - 1)) : 0x40000000; };
  int tbase = w * 8192;

  u32x4 ring[8][4];
  u32x4 qf[4], tk[4], tv[4];
  u32x2 mk;
  asm volatile("buffer_load_dwordx4 %0, %4, %5, 0 offen\n\tbuffer_load_dwordx4 %1, %4, %5, 0 offen offset:64\n\t"
               "buffer_load_dwordx4 %2, %4, %5, 0 offen offset:128\n\tbuffer_load_dwordx4 %3, %4, %5, 0 offen offset:192"
               : "=&v"(qf[0]), "=&v"(qf[1]), "=&v"(qf[2]), "=&v"(qf[3]) : "v"(qoff), "s"(rq) : "memory");
  if (w == 1) {
    const u32x4 rtk = mkrs(kt, (size_t)bh * (Tn * Dn * 2), Tn * Dn * 2);
    const u32x4 rtv = mkrs(vt, (size_t)bh * (Tn * Dn * 2), Tn * Dn * 2);
    const u32x4 rm = mkrs(mask, (size_t)(bh / Hn) * (Tn * Tn * 2), Tn * Tn * 2);
    const int tvoff = g * 1024 + r * 16, moff = r * 32 + g * 8;
    asm volatile("buffer_load_dwordx4 %0, %4, %5, 0 offen\n\tbuffer_load_dwordx4 %1, %4, %5, 0 offen offset:64\n\t"
                 "buffer_load_dwordx4 %2, %4, %5, 0 offen offset:128\n\tbuffer_load_dwordx4 %3, %4, %5, 0 offen offset:192"
                 : "=&v"(tk[0]), "=&v"(tk[1]), "=&v"(tk[2]), "=&v"(tk[3]) : "v"(qoff), "s"(rtk) : "memory");
    asm volatile("buffer_load_dwordx4 %0, %4, %5, 0 offen\n\tbuffer_load_dwordx4 %1, %4, %5, 0 offen offset:256\n\t"
                 "buffer_load_dwordx4 %2, %4, %5, 0 offen offset:512\n\tbuffer_load_dwordx4 %3, %4, %5, 0 offen offset:768"
                 : "=&v"(tv[0]), "=&v"(tv[1]), "=&v"(tv[2]), "=&v"(tv[3]) : "v"(tvoff), "s"(rtv) : "memory");
    asm volatile("buffer_load_dwordx2 %0, %1, %2, 0 offen" : "=&v"(mk) : "v"(moff), "s"(rm) : "memory");
  }
  {
    const int so0 = toff(tbase);
    issue<0, true>(ring[0], rk, rv, koff, voff, so0);
    issue<1, true>(ring[1], rk, rv, koff, voff, so0);
    issue<2, true>(ring[2], rk, rv, koff, voff, so0);
    issue<3, true>(ring[3], rk, rv, koff, voff, so0);
  }
#pragma unroll
  for (int s = 4; s < 8; ++s)
#pragma unroll
    for (int k = 0; k < 4; ++k) asm volatile("" : "=v"(ring[s][k]));
  SB();

  State st;
  st.m = -INFINITY; st.l = 0.f;
#pragma unroll
  for (int n = 0; n < 8; ++n) st.acc[n] = f32x4{0.f, 0.f, 0.f, 0.f};
  st.s0 = f32x4{0.f, 0.f, 0.f, 0.f}; st.pb = u32x4{0, 0, 0, 0};
  asm volatile("s_waitcnt vmcnt(16)" : "+v"(qf[0]), "+v"(qf[1]), "+v"(qf[2]), "+v"(qf[3]) :: "memory");
  if (w == 1) {
    asm volatile("" : "+v"(tk[0]), "+v"(tk[1]), "+v"(tk[2]), "+v"(tk[3]), "+v"(tv[0]), "+v"(tv[1]), "+v"(tv[2]), "+v"(tv[3]), "+v"(mk) :: "memory");
    f32x4 s0 = {0.f, 0.f, 0.f, 0.f};
#pragma unroll
    for (int s = 0; s < 4; ++s) s0 = mfma32(tk[s], qf[s], s0);
    const float mv[4] = {__uint_as_float(mk[0] << 16), __uint_as_float(mk[0] & 0xffff0000u), __uint_as_float(mk[1] << 16), __uint_as_float(mk[1] & 0xffff0000u)};
    float x[4];
#pragma unroll
    for (int i = 0; i < 4; ++i) x[i] = fmaf(s0[i], CS, (1.0f - mv[i]) * (-1.0e9f * LOG2E));
    st.m = xrow_max(fmaxf(fmaxf(x[0], x[1]), fmaxf(x[2], x[3])));
    float p[4];
#pragma unroll
    for (int i = 0; i < 4; ++i) p[i] = __builtin_amdgcn_exp2f(x[i] - st.m);
    st.l = (p[0] + p[1]) + (p[2] + p[3]);
    const u32x2 pb = {cvtpk(p[0], p[1]), cvtpk(p[2], p[3])};
#pragma unroll
    for (int wd = 0; wd < 4; ++wd) {
      const u32x2 a0 = {perm_lo(tv[1][wd], tv[0][wd]), perm_lo(tv[3][wd], tv[2][wd])};
      const u32x2 a1 = {perm_hi(tv[1][wd], tv[0][wd]), perm_hi(tv[3][wd], tv[2][wd])};
      st.acc[2 * wd] = mfma16(a0, pb, st.acc[2 * wd]);
      st.acc[2 * wd + 1] = mfma16(a1, pb, st.acc[2 * wd + 1]);
    }
  }
  SB();

#pragma unroll 1
  for (int i = 0; i < TPW; i += 2) {
    const int so[3] = {0, toff(tbase + tstep), toff(tbase + 2 * tstep)};
    consume<0>(ring, qf, st, rk, rv, koff, voff, so);
    consume<1>(ring, qf, st, rk, rv, koff, voff, so);
    consume<2>(ring, qf, st, rk, rv, koff, voff, so);
    consume<3>(ring, qf, st, rk, rv, koff, voff, so);
    consume<4>(ring, qf, st, rk, rv, koff, voff, so);
    consume<5>(ring, qf, st, rk, rv, koff, voff, so);
    consume<6>(ring, qf, st, rk, rv, koff, voff, so);
    consume<7>(ring, qf, st, rk, rv, koff, voff, so);
    tbase += 2 * tstep;
  }
  wait_pair<0>(ring[0], ring[1]);
  wait_pair<0>(ring[2], ring[3]);

  const float lsum = xrow_sum(st.l);
  unsigned short* orow = out + ((size_t)bh * Tn + r) * Dn + 32 * g;
  if (w == 0) merge_send<0>(st, lsum, smem, lane);
  else        merge_send<1>(st, lsum, smem, lane);
  __syncthreads();
  if (w == 0) merge_recv<0>(st, lsum, smem, lane, orow);
  else        merge_recv<1>(st, lsum, smem, lane, orow);
}

extern "C" void kernel_launch(void* const* d_in, const int* in_sizes, int n_in, void* d_out, int out_size, void* d_ws, size_t ws_size,
                              hipStream_t stream) {
  (void)in_sizes; (void)n_in; (void)out_size; (void)d_ws; (void)ws_size;
  (void)hipFuncSetAttribute((const void*)tree_attn_kernel, hipFuncAttributeMaxDynamicSharedMemorySize, LDS_BYTES);
  tree_attn_kernel<<<dim3(Bn * Hn), dim3(NW * 64), LDS_BYTES, stream>>>(
      (const unsigned short*)d_in[0], (const unsigned short*)d_in[1], (const unsigned short*)d_in[2], (const unsigned short*)d_in[3],
      (const unsigned short*)d_in[4], (const unsigned short*)d_in[5], (unsigned short*)d_out);
}
```

```cpp
#include <hip/hip_runtime.h>
#include <stdint.h>

typedef unsigned u32x4 __attribute__((ext_vector_type(4)));
typedef unsigned u32x2 __attribute__((ext_vector_type(2)));
typedef float f32x4 __attribute__((ext_vector_type(4)));
typedef float f32x2 __attribute__((ext_vector_type(2)));
typedef __bf16 bf16x8 __attribute__((ext_vector_type(8)));
typedef __bf16 bf16x2 __attribute__((ext_vector_type(2)));
typedef short s16x4 __attribute__((ext_vector_type(4)));

constexpr int Bn = 8, Hn = 32, Tn = 16, Cn = 4096, Dn = 128;
constexpr int NW = 2;
constexpr int NTILE = Cn / 32;
constexpr int TPW = NTILE / NW;
constexpr float SCALE = 0.08838834764831845f;
constexpr float LOG2E = 1.4426950408889634f;
constexpr float CS = SCALE * LOG2E;
constexpr int LDS_BYTES = 84 * 1024;
constexpr int HEAD_BYTES = Cn * Dn * 2;
static_assert(TPW % 2 == 0, "shape");

__device__ __forceinline__ f32x4 mfma32(u32x4 a, u32x4 b, f32x4 c) {
  return __builtin_amdgcn_mfma_f32_16x16x32_bf16(__builtin_bit_cast(bf16x8, a), __builtin_bit_cast(bf16x8, b), c, 0, 0, 0);
}
__device__ __forceinline__ f32x4 mfma16(u32x2 a, u32x2 b, f32x4 c) {
  return __builtin_amdgcn_mfma_f32_16x16x16bf16_1k(__builtin_bit_cast(s16x4, a), __builtin_bit_cast(s16x4, b), c, 0, 0, 0);
}
__device__ __forceinline__ float xrow_max(float x) {
  auto s = __builtin_amdgcn_permlane16_swap(__float_as_uint(x), __float_as_uint(x), false, false);
  x = fmaxf(__uint_as_float(s[0]), __uint_as_float(s[1]));
  auto t = __builtin_amdgcn_permlane32_swap(__float_as_uint(x), __float_as_uint(x), false, false);
  return fmaxf(__uint_as_float(t[0]), __uint_as_float(t[1]));
}
__device__ __forceinline__ float xrow_sum(float x) {
  auto s = __builtin_amdgcn_permlane16_swap(__float_as_uint(x), __float_as_uint(x), false, false);
  x = __uint_as_float(s[0]) + __uint_as_float(s[1]);
  auto t = __builtin_amdgcn_permlane32_swap(__float_as_uint(x), __float_as_uint(x), false, false);
  return __uint_as_float(t[0]) + __uint_as_float(t[1]);
}
__device__ __forceinline__ unsigned cvtpk(float lo, float hi) {
  const f32x2 v = {lo, hi};
  return __builtin_bit_cast(unsigned, __builtin_convertvector(v, bf16x2));
}
__device__ __forceinline__ unsigned perm_lo(unsigned hi, unsigned lo) { return __builtin_amdgcn_perm(hi, lo, 0x05040100u); }
__device__ __forceinline__ unsigned perm_hi(unsigned hi, unsigned lo) { return __builtin_amdgcn_perm(hi, lo, 0x07060302u); }
__device__ __forceinline__ u32x4 mkrs(const void* p, size_t byte_off, unsigned bytes) {
  const uint64_t a = (uint64_t)p + byte_off;
  return u32x4{(unsigned)a, (unsigned)(a >> 32) & 0xffffu, bytes, 0x00020000u};
}
#define SB() __builtin_amdgcn_sched_barrier(0)

template <int IMM, bool FIRST> __device__ __forceinline__ void bload(u32x4& d, int voff, u32x4 rs, int soff) {
  if constexpr (FIRST) asm volatile("buffer_load_dwordx4 %0, %1, %2, %3 offen offset:%c4 nt" : "=v"(d) : "v"(voff), "s"(rs), "s"(soff), "i"(IMM) : "memory");
  else                 asm volatile("buffer_load_dwordx4 %0, %1, %2, %3 offen offset:%c4 nt" : "+v"(d) : "v"(voff), "s"(rs), "s"(soff), "i"(IMM) : "memory");
}
template <int N> __device__ __forceinline__ void wait_pair(u32x4 (&a)[4], u32x4 (&b)[4]) {
  asm volatile("s_waitcnt vmcnt(%c8)" : "+v"(a[0]), "+v"(a[1]), "+v"(a[2]), "+v"(a[3]), "+v"(b[0]), "+v"(b[1]), "+v"(b[2]), "+v"(b[3]) : "i"(N) : "memory");
}
template <int KIND, bool FIRST> __device__ __forceinline__ void issue(u32x4 (&d)[4], u32x4 rk, u32x4 rv, int koff, int voff, int so) {
  if constexpr (KIND < 2) {
    const int s = so + KIND * 4096;
    bload<0, FIRST>(d[0], koff, rk, s); bload<64, FIRST>(d[1], koff, rk, s); bload<128, FIRST>(d[2], koff, rk, s); bload<192, FIRST>(d[3], koff, rk, s);
  } else {
    const int s = so + (KIND - 2) * 4096;
    bload<0, FIRST>(d[0], voff, rv, s); bload<1024, FIRST>(d[1], voff, rv, s); bload<2048, FIRST>(d[2], voff, rv, s); bload<3072, FIRST>(d[3], voff, rv, s);
  }
}

struct State { float m, l; f32x4 acc[8]; u32x4 pb; };

template <int C> __device__ __forceinline__ void consume(u32x4 (&ring)[8][4], const u32x4 (&qf)[4], State& st, u32x4 rk, u32x4 rv, int koff, int voff, const int (&so)[2]) {
  wait_pair<8>(ring[C], ring[C + 1]);
  SB();
  issue<C % 4, false>(ring[(C + 4) % 8], rk, rv, koff, voff, so[C / 4]);
  issue<C % 4 + 1, false>(ring[(C + 5) % 8], rk, rv, koff, voff, so[C / 4]);
  SB();
  if constexpr (C % 4 == 0) {
    f32x4 s0 = {0.f, 0.f, 0.f, 0.f}, s1 = {0.f, 0.f, 0.f, 0.f};
#pragma unroll
    for (int k = 0; k < 4; ++k) { s0 = mfma32(ring[C][k], qf[k], s0); s1 = mfma32(ring[C + 1][k], qf[k], s1); }
    float tm = fmaxf(fmaxf(fmaxf(s0[0], s0[1]), fmaxf(s0[2], s0[3])), fmaxf(fmaxf(s1[0], s1[1]), fmaxf(s1[2], s1[3])));
    tm = xrow_max(tm) * CS;
    const float mn = fmaxf(st.m, tm);
    const float alpha = __builtin_amdgcn_exp2f(st.m - mn);
    st.l *= alpha;
#pragma unroll
    for (int n = 0; n < 8; ++n) st.acc[n] *= alpha;
    st.m = mn;
    float p[8];
#pragma unroll
    for (int i = 0; i < 4; ++i) { p[i] = __builtin_amdgcn_exp2f(fmaf(s0[i], CS, -mn)); p[4 + i] = __builtin_amdgcn_exp2f(fmaf(s1[i], CS, -mn)); }
    st.l += ((p[0] + p[1]) + (p[2] + p[3])) + ((p[4] + p[5]) + (p[6] + p[7]));
    st.pb = u32x4{cvtpk(p[0], p[1]), cvtpk(p[2], p[3]), cvtpk(p[4], p[5]), cvtpk(p[6], p[7])};
  } else {
    u32x4(&va)[4] = ring[C];
    u32x4(&vb)[4] = ring[C + 1];
#pragma unroll
    for (int wd = 0; wd < 4; ++wd) {
      const u32x4 a0 = {perm_lo(va[1][wd], va[0][wd]), perm_lo(va[3][wd], va[2][wd]), perm_lo(vb[1][wd], vb[0][wd]), perm_lo(vb[3][wd], vb[2][wd])};
      const u32x4 a1 = {perm_hi(va[1][wd], va[0][wd]), perm_hi(va[3][wd], va[2][wd]), perm_hi(vb[1][wd], vb[0][wd]), perm_hi(vb[3][wd], vb[2][wd])};
      st.acc[2 * wd] = mfma32(a0, st.pb, st.acc[2 * wd]);
      st.acc[2 * wd + 1] = mfma32(a1, st.pb, st.acc[2 * wd + 1]);
    }
  }
  SB();
}

template <int W> __device__ __forceinline__ void merge_send(const State& st, float lsum, unsigned char* smem, int lane) {
  f32x4* xo = reinterpret_cast<f32x4*>(smem);
  f32x2* xm = reinterpret_cast<f32x2*>(smem + 8192);
  constexpr int S = 2 * (1 - W);
#pragma unroll
  for (int j = 0; j < 2; ++j) {
    xo[(W * 4 + 2 * j) * 64 + lane] = f32x4{st.acc[0][S + j], st.acc[1][S + j], st.acc[2][S + j], st.acc[3][S + j]};
    xo[(W * 4 + 2 * j + 1) * 64 + lane] = f32x4{st.acc[4][S + j], st.acc[5][S + j], st.acc[6][S + j], st.acc[7][S + j]};
  }
  xm[W * 64 + lane] = f32x2{st.m, lsum};
}
template <int W> __device__ __forceinline__ void merge_recv(const State& st, float lsum, const unsigned char* smem, int lane, unsigned short* orow) {
  const f32x4* xo = reinterpret_cast<const f32x4*>(smem);
  const f32x2* xm = reinterpret_cast<const f32x2*>(smem + 8192);
  constexpr int K = 2 * W;
  const f32x2 o = xm[(1 - W) * 64 + lane];
  const float M = fmaxf(st.m, o[0]);
  float a = __builtin_amdgcn_exp2f(st.m - M), b = __builtin_amdgcn_exp2f(o[0] - M);
  const float inv = 1.0f / fmaf(lsum, a, o[1] * b);
  a *= inv; b *= inv;
#pragma unroll
  for (int j = 0; j < 2; ++j) {
    const f32x4 x0 = xo[((1 - W) * 4 + 2 * j) * 64 + lane], x1 = xo[((1 - W) * 4 + 2 * j + 1) * 64 + lane];
    const u32x4 ob = {cvtpk(fmaf(st.acc[0][K + j], a, x0[0] * b), fmaf(st.acc[1][K + j], a, x0[1] * b)),
                      cvtpk(fmaf(st.acc[2][K + j], a, x0[2] * b), fmaf(st.acc[3][K + j], a, x0[3] * b)),
                      cvtpk(fmaf(st.acc[4][K + j], a, x1[0] * b), fmaf(st.acc[5][K + j], a, x1[1] * b)),
                      cvtpk(fmaf(st.acc[6][K + j], a, x1[2] * b), fmaf(st.acc[7][K + j], a, x1[3] * b))};
    *reinterpret_cast<u32x4*>(orow + 8 * (K + j)) = ob;
  }
}

__global__ void __launch_bounds__(NW * 64, 2)
tree_attn_kernel(const unsigned short* __restrict__ q, const unsigned short* __restrict__ kc, const unsigned short* __restrict__ vc,
                 const unsigned short* __restrict__ kt, const unsigned short* __restrict__ vt, const unsigned short* __restrict__ mask,
                 unsigned short* __restrict__ out) {
  extern __shared__ __attribute__((aligned(16))) unsigned char smem[];
  asm volatile("" :: "s"(q), "s"(kc), "s"(vc), "s"(kt), "s"(vt), "s"(mask), "s"(out));
  const int bh = (blockIdx.x & 7) * 32 + (blockIdx.x >> 3);
  const int tid = threadIdx.x, lane = tid & 63, w = __builtin_amdgcn_readfirstlane(tid >> 6);
  const int r = lane & 15, g = lane >> 4;

  const int koff = (4 * (r & 3) + (r >> 2)) * 256 + g * 16;
  const int voff = lane * 16;
  const int qoff = r * 256 + g * 16;
  const u32x4 rk = mkrs(kc, (size_t)bh * HEAD_BYTES, HEAD_BYTES);
  const u32x4 rv = mkrs(vc, (size_t)bh * HEAD_BYTES, HEAD_BYTES);
  const u32x4 rq = mkrs(q, (size_t)bh * (Tn * Dn * 2), Tn * Dn * 2);
  const int tstep = NW * 8192;
  const int rot = (bh & 1) * (HEAD_BYTES / 2);
  auto toff = [rot](int x) -> int { return x < HEAD_BYTES ? ((x + rot) & (HEAD_BYTES - 1)) : 0x40000000; };
  int tbase = w * 8192;

  u32x4 ring[8][4];
  u32x4 qf[4], tk[4], tv[4];
  u32x2 mk;
  asm volatile("buffer_load_dwordx4 %0, %4, %5, 0 offen\n\tbuffer_load_dwordx4 %1, %4, %5, 0 offen offset:64\n\t"
               "buffer_load_dwordx4 %2, %4, %5, 0 offen offset:128\n\tbuffer_load_dwordx4 %3, %4, %5, 0 offen offset:192"
               : "=&v"(qf[0]), "=&v"(qf[1]), "=&v"(qf[2]), "=&v"(qf[3]) : "v"(qoff), "s"(rq) : "memory");
  if (w == 1) {
    const u32x4 rtk = mkrs(kt, (size_t)bh * (Tn * Dn * 2), Tn * Dn * 2);
    const u32x4 rtv = mkrs(vt, (size_t)bh * (Tn * Dn * 2), Tn * Dn * 2);
    const u32x4 rm = mkrs(mask, (size_t)(bh / Hn) * (Tn * Tn * 2), Tn * Tn * 2);
    const int tvoff = g * 1024 + r * 16, moff = r * 32 + g * 8;
    asm volatile("buffer_load_dwordx4 %0, %4, %5, 0 offen\n\tbuffer_load_dwordx4 %1, %4, %5, 0 offen offset:64\n\t"
                 "buffer_load_dwordx4 %2, %4, %5, 0 offen offset:128\n\tbuffer_load_dwordx4 %3, %4, %5, 0 offen offset:192"
                 : "=&v"(tk[0]), "=&v"(tk[1]), "=&v"(tk[2]), "=&v"(tk[3]) : "v"(qoff), "s"(rtk) : "memory");
    asm volatile("buffer_load_dwordx4 %0, %4, %5, 0 offen\n\tbuffer_load_dwordx4 %1, %4, %5, 0 offen offset:256\n\t"
                 "buffer_load_dwordx4 %2, %4, %5, 0 offen offset:512\n\tbuffer_load_dwordx4 %3, %4, %5, 0 offen offset:768"
                 : "=&v"(tv[0]), "=&v"(tv[1]), "=&v"(tv[2]), "=&v"(tv[3]) : "v"(tvoff), "s"(rtv) : "memory");
    asm volatile("buffer_load_dwordx2 %0, %1, %2, 0 offen" : "=&v"(mk) : "v"(moff), "s"(rm) : "memory");
  }
  {
    const int so0 = toff(tbase);
    issue<0, true>(ring[0], rk, rv, koff, voff, so0);
    issue<1, true>(ring[1], rk, rv, koff, voff, so0);
    issue<2, true>(ring[2], rk, rv, koff, voff, so0);
    issue<3, true>(ring[3], rk, rv, koff, voff, so0);
  }
#pragma unroll
  for (int s = 4; s < 8; ++s)
#pragma unroll
    for (int k = 0; k < 4; ++k) asm volatile("" : "=v"(ring[s][k]));
  SB();

  State st;
  st.m = -INFINITY; st.l = 0.f;
#pragma unroll
  for (int n = 0; n < 8; ++n) st.acc[n] = f32x4{0.f, 0.f, 0.f, 0.f};
  st.pb = u32x4{0, 0, 0, 0};
  asm volatile("s_waitcnt vmcnt(16)" : "+v"(qf[0]), "+v"(qf[1]), "+v"(qf[2]), "+v"(qf[3]) :: "memory");
  if (w == 1) {
    asm volatile("" : "+v"(tk[0]), "+v"(tk[1]), "+v"(tk[2]), "+v"(tk[3]), "+v"(tv[0]), "+v"(tv[1]), "+v"(tv[2]), "+v"(tv[3]), "+v"(mk) :: "memory");
    f32x4 s0 = {0.f, 0.f, 0.f, 0.f};
#pragma unroll
    for (int s = 0; s < 4; ++s) s0 = mfma32(tk[s], qf[s], s0);
    const float mv[4] = {__uint_as_float(mk[0] << 16), __uint_as_float(mk[0] & 0xffff0000u), __uint_as_float(mk[1] << 16), __uint_as_float(mk[1] & 0xffff0000u)};
    float x[4];
#pragma unroll
    for (int i = 0; i < 4; ++i) x[i] = fmaf(s0[i], CS, (1.0f - mv[i]) * (-1.0e9f * LOG2E));
    st.m = xrow_max(fmaxf(fmaxf(x[0], x[1]), fmaxf(x[2], x[3])));
    float p[4];
#pragma unroll
    for (int i = 0; i < 4; ++i) p[i] = __builtin_amdgcn_exp2f(x[i] - st.m);
    st.l = (p[0] + p[1]) + (p[2] + p[3]);
    const u32x2 pb = {cvtpk(p[0], p[1]), cvtpk(p[2], p[3])};
#pragma unroll
    for (int wd = 0; wd < 4; ++wd) {
      const u32x2 a0 = {perm_lo(tv[1][wd], tv[0][wd]), perm_lo(tv[3][wd], tv[2][wd])};
      const u32x2 a1 = {perm_hi(tv[1][wd], tv[0][wd]), perm_hi(tv[3][wd], tv[2][wd])};
      st.acc[2 * wd] = mfma16(a0, pb, st.acc[2 * wd]);
      st.acc[2 * wd + 1] = mfma16(a1, pb, st.acc[2 * wd + 1]);
    }
  }
  SB();

#pragma unroll 1
  for (int i = 0; i < TPW; i += 2) {
    const int so[2] = {toff(tbase + tstep), toff(tbase + 2 * tstep)};
    consume<0>(ring, qf, st, rk, rv, koff, voff, so);
    consume<2>(ring, qf, st, rk, rv, koff, voff, so);
    consume<4>(ring, qf, st, rk, rv, koff, voff, so);
    consume<6>(ring, qf, st, rk, rv, koff, voff, so);
    tbase += 2 * tstep;
  }
  wait_pair<0>(ring[0], ring[1]);
  wait_pair<0>(ring[2], ring[3]);

  const float lsum = xrow_sum(st.l);
  unsigned short* orow = out + ((size_t)bh * Tn + r) * Dn + 32 * g;
  if (w == 0) merge_send<0>(st, lsum, smem, lane);
  else        merge_send<1>(st, lsum, smem, lane);
  __syncthreads();
  if (w == 0) merge_recv<0>(st, lsum, smem, lane, orow);
  else        merge_recv<1>(st, lsum, smem, lane, orow);
}

extern "C" void kernel_launch(void* const* d_in, const int* in_sizes, int n_in, void* d_out, int out_size, void* d_ws, size_t ws_size,
                              hipStream_t stream) {
  (void)in_sizes; (void)n_in; (void)out_size; (void)d_ws; (void)ws_size;
  (void)hipFuncSetAttribute((const void*)tree_attn_kernel, hipFuncAttributeMaxDynamicSharedMemorySize, LDS_BYTES);
  tree_attn_kernel<<<dim3(Bn * Hn), dim3(NW * 64), LDS_BYTES, stream>>>(
      (const unsigned short*)d_in[0], (const unsigned short*)d_in[1], (const unsigned short*)d_in[2], (const unsigned short*)d_in[3],
      (const unsigned short*)d_in[4], (const unsigned short*)d_in[5], (unsigned short*)d_out);
}
```

```cpp
#include <hip/hip_runtime.h>
#include <stdint.h>

typedef unsigned u32x4 __attribute__((ext_vector_type(4)));
typedef unsigned u32x2 __attribute__((ext_vector_type(2)));
typedef float f32x4 __attribute__((ext_vector_type(4)));
typedef float f32x2 __attribute__((ext_vector_type(2)));
typedef __bf16 bf16x8 __attribute__((ext_vector_type(8)));
typedef __bf16 bf16x2 __attribute__((ext_vector_type(2)));
typedef short s16x4 __attribute__((ext_vector_type(4)));
#define LAS __attribute__((address_space(3)))

constexpr int Bn = 8, Hn = 32, Tn = 16, Cn = 4096, Dn = 128;
constexpr int NW = 2;
constexpr int TPW = Cn / 32 / NW;
constexpr int RUK = 13, RUV = 11;
constexpr float SCALE = 0.08838834764831845f;
constexpr float LOG2E = 1.4426950408889634f;
constexpr float CS = SCALE * LOG2E;
constexpr int HEAD_BYTES = Cn * Dn * 2;
constexpr int LDS_P = 0, LDS_A = NW * TPW * 1024, LDS_S = LDS_A + NW * TPW * 64, LDS_BYTES = LDS_S + NW * 8192;
static_assert(LDS_BYTES <= 160 * 1024 && RUV <= RUK && RUK <= 16, "a slot is reused 32 loads later: at most 24 in flight; V's first 2 tiles start in the K phase");

__device__ __forceinline__ f32x4 mfma32(u32x4 a, u32x4 b, f32x4 c) {
  return __builtin_amdgcn_mfma_f32_16x16x32_bf16(__builtin_bit_cast(bf16x8, a), __builtin_bit_cast(bf16x8, b), c, 0, 0, 0);
}
__device__ __forceinline__ f32x4 mfma16(u32x2 a, u32x2 b, f32x4 c) {
  return __builtin_amdgcn_mfma_f32_16x16x16bf16_1k(__builtin_bit_cast(s16x4, a), __builtin_bit_cast(s16x4, b), c, 0, 0, 0);
}
__device__ __forceinline__ float xrow_max(float x) {
  auto s = __builtin_amdgcn_permlane16_swap(__float_as_uint(x), __float_as_uint(x), false, false);
  x = fmaxf(__uint_as_float(s[0]), __uint_as_float(s[1]));
  auto t = __builtin_amdgcn_permlane32_swap(__float_as_uint(x), __float_as_uint(x), false, false);
  return fmaxf(__uint_as_float(t[0]), __uint_as_float(t[1]));
}
__device__ __forceinline__ float xrow_sum(float x) {
  auto s = __builtin_amdgcn_permlane16_swap(__float_as_uint(x), __float_as_uint(x), false, false);
  x = __uint_as_float(s[0]) + __uint_as_float(s[1]);
  auto t = __builtin_amdgcn_permlane32_swap(__float_as_uint(x), __float_as_uint(x), false, false);
  return __uint_as_float(t[0]) + __uint_as_float(t[1]);
}
__device__ __forceinline__ unsigned cvtpk(float lo, float hi) {
  const f32x2 v = {lo, hi};
  return __builtin_bit_cast(unsigned, __builtin_convertvector(v, bf16x2));
}
__device__ __forceinline__ unsigned perm_lo(unsigned hi, unsigned lo) { return __builtin_amdgcn_perm(hi, lo, 0x05040100u); }
__device__ __forceinline__ unsigned perm_hi(unsigned hi, unsigned lo) { return __builtin_amdgcn_perm(hi, lo, 0x07060302u); }
__device__ __forceinline__ u32x4 mkrs(const void* p, size_t byte_off, unsigned bytes) {
  const uint64_t a = (uint64_t)p + byte_off;
  return u32x4{(unsigned)a, (unsigned)(a >> 32) & 0xffffu, bytes, 0x00020000u};
}
#define SB() __builtin_amdgcn_sched_barrier(0)

template <int IMM, bool FIRST> __device__ __forceinline__ void bload(u32x4& d, int voff, u32x4 rs, int soff) {
  if constexpr (FIRST) asm volatile("buffer_load_dwordx4 %0, %1, %2, %3 offen offset:%c4 nt" : "=v"(d) : "v"(voff), "s"(rs), "s"(soff), "i"(IMM) : "memory");
  else                 asm volatile("buffer_load_dwordx4 %0, %1, %2, %3 offen offset:%c4 nt" : "+v"(d) : "v"(voff), "s"(rs), "s"(soff), "i"(IMM) : "memory");
}
template <int N> __device__ __forceinline__ void wait1(u32x4& d) { asm volatile("s_waitcnt vmcnt(%c1)" : "+v"(d) : "i"(N) : "memory"); }

template <bool FRAG, int J, bool FIRST> __device__ __forceinline__ void issue(u32x4& d, int voff, int koff, u32x4 rs, int so) {
  if constexpr (FRAG) bload<(J & 3) * 64, FIRST>(d, koff, rs, so + (J >> 2) * 4096);
  else                bload<(J & 3) * 1024, FIRST>(d, voff, rs, so + (J >> 2) * 4096);
}
constexpr bool direct_tile(int rel) { return rel % 4 == 0; }

constexpr int NTL = (31 + RUK) / 8 + 1;
struct Tiles { u32x4 rs[NTL]; int so[NTL]; };
struct Lds { LAS unsigned char* base; int wr[4], rd[4], pw, aw; };
struct State { float m, l; f32x4 acc[8]; };

template <int C, bool LAST> __device__ __forceinline__ void kstep(u32x4 (&ring)[32], const Tiles& tl, int voff, int koff, const u32x4 (&qf)[4], State& st, const Lds& L, int tile0) {
  wait1<RUK - 1>(ring[C]);
  SB();
  constexpr int X = C + RUK;
  issue<direct_tile((X / 8) % 4) && !(LAST && X >= 32), X % 8, false>(ring[X % 32], voff, koff, tl.rs[X / 8], tl.so[X / 8]);
  SB();
  constexpr int J = C % 8;
  constexpr bool DIR = direct_tile(C / 8);
  if constexpr (!DIR) *reinterpret_cast<LAS u32x4*>(L.base + L.wr[J & 3] + (J >> 2) * 4096 + (J & 3) * 1024) = ring[C];
  if constexpr (J == 7) {
    f32x4 s0 = {0.f, 0.f, 0.f, 0.f}, s1 = {0.f, 0.f, 0.f, 0.f};
#pragma unroll
    for (int s = 0; s < 4; ++s) {
      if constexpr (DIR) { s0 = mfma32(ring[C - 7 + s], qf[s], s0); s1 = mfma32(ring[C - 3 + s], qf[s], s1); }
      else {
        const u32x4 k0 = *reinterpret_cast<const LAS u32x4*>(L.base + L.rd[s]);
        const u32x4 k1 = *reinterpret_cast<const LAS u32x4*>(L.base + L.rd[s] + 4096);
        s0 = mfma32(k0, qf[s], s0); s1 = mfma32(k1, qf[s], s1);
      }
    }
    float tm = fmaxf(fmaxf(fmaxf(s0[0], s0[1]), fmaxf(s0[2], s0[3])), fmaxf(fmaxf(s1[0], s1[1]), fmaxf(s1[2], s1[3])));
    tm = xrow_max(tm) * CS;
    const float mn = fmaxf(st.m, tm);
    const float alpha = __builtin_amdgcn_exp2f(st.m - mn);
    st.m = mn;
    float p[8];
#pragma unroll
    for (int i = 0; i < 4; ++i) { p[i] = __builtin_amdgcn_exp2f(fmaf(s0[i], CS, -mn)); p[4 + i] = __builtin_amdgcn_exp2f(fmaf(s1[i], CS, -mn)); }
    st.l = fmaf(st.l, alpha, ((p[0] + p[1]) + (p[2] + p[3])) + ((p[4] + p[5]) + (p[6] + p[7])));
    asm volatile("" : "+v"(st.l));
    const int t = tile0 + C / 8;
    *reinterpret_cast<LAS u32x4*>(L.base + L.pw + t * 1024) = u32x4{cvtpk(p[0], p[1]), cvtpk(p[2], p[3]), cvtpk(p[4], p[5]), cvtpk(p[6], p[7])};
    *reinterpret_cast<LAS float*>(L.base + L.aw + t * 64) = alpha;
  }
  SB();
}
template <int C, bool FIRST> __device__ __forceinline__ void vstep(u32x4 (&ring)[32], const Tiles& tl, int voff, State& st, const Lds& L, int tile0) {
  if constexpr (FIRST && C + RUV < RUK) {
    wait1<RUK - 1 - C>(ring[C]);
  } else {
    wait1<RUV - 1>(ring[C]);
    SB();
    issue<false, (C + RUV) % 8, false>(ring[(C + RUV) % 32], voff, 0, tl.rs[(C + RUV) / 8], tl.so[(C + RUV) / 8]);
  }
  SB();
  if constexpr (C % 8 == 7) {
    const int t = tile0 + C / 8;
    const u32x4 pb = *reinterpret_cast<const LAS u32x4*>(L.base + L.pw + t * 1024);
    const float alpha = *reinterpret_cast<const LAS float*>(L.base + L.aw + t * 64);
#pragma unroll
    for (int n = 0; n < 8; ++n) st.acc[n] *= alpha;
    const u32x4* v = &ring[C - 7];
#pragma unroll
    for (int wd = 0; wd < 4; ++wd) {
      const u32x4 a0 = {perm_lo(v[1][wd], v[0][wd]), perm_lo(v[3][wd], v[2][wd]), perm_lo(v[5][wd], v[4][wd]), perm_lo(v[7][wd], v[6][wd])};
      const u32x4 a1 = {perm_hi(v[1][wd], v[0][wd]), perm_hi(v[3][wd], v[2][wd]), perm_hi(v[5][wd], v[4][wd]), perm_hi(v[7][wd], v[6][wd])};
      st.acc[2 * wd] = mfma32(a0, pb, st.acc[2 * wd]);
      st.acc[2 * wd + 1] = mfma32(a1, pb, st.acc[2 * wd + 1]);
    }
  }
  SB();
}
template <int C, bool LAST> __device__ __forceinline__ void kbody(u32x4 (&ring)[32], const Tiles& tl, int voff, int koff, const u32x4 (&qf)[4], State& st, const Lds& L, int tile0) {
  if constexpr (C < 32) { kstep<C, LAST>(ring, tl, voff, koff, qf, st, L, tile0); kbody<C + 1, LAST>(ring, tl, voff, koff, qf, st, L, tile0); }
}
template <int C, bool FIRST> __device__ __forceinline__ void vbody(u32x4 (&ring)[32], const Tiles& tl, int voff, State& st, const Lds& L, int tile0) {
  if constexpr (C < 32) { vstep<C, FIRST>(ring, tl, voff, st, L, tile0); vbody<C + 1, FIRST>(ring, tl, voff, st, L, tile0); }
}
template <int C> __device__ __forceinline__ void prime(u32x4 (&ring)[32], int voff, int koff, u32x4 rk, int so0, int so1) {
  if constexpr (C < RUK) { issue<direct_tile(C / 8), C % 8, true>(ring[C], voff, koff, rk, C < 8 ? so0 : so1); prime<C + 1>(ring, voff, koff, rk, so0, so1); }
}
template <int C> __device__ __forceinline__ void drain(u32x4 (&ring)[32]) {
  if constexpr (C < RUV) { wait1<0>(ring[C]); drain<C + 1>(ring); }
}

template <int W> __device__ __forceinline__ void merge_send(const State& st, float lsum, unsigned char* smem, int lane) {
  f32x4* xo = reinterpret_cast<f32x4*>(smem + LDS_P + W * (TPW * 1024));
  f32x2* xm = reinterpret_cast<f32x2*>(smem + LDS_P + W * (TPW * 1024) + 4096);
  constexpr int S = 2 * (1 - W);
#pragma unroll
  for (int j = 0; j < 2; ++j) {
    xo[(2 * j) * 64 + lane] = f32x4{st.acc[0][S + j], st.acc[1][S + j], st.acc[2][S + j], st.acc[3][S + j]};
    xo[(2 * j + 1) * 64 + lane] = f32x4{st.acc[4][S + j], st.acc[5][S + j], st.acc[6][S + j], st.acc[7][S + j]};
  }
  xm[lane] = f32x2{st.m, lsum};
}
template <int W> __device__ __forceinline__ void merge_recv(const State& st, float lsum, const unsigned char* smem, int lane, unsigned short* orow) {
  const f32x4* xo = reinterpret_cast<const f32x4*>(smem + LDS_P + (1 - W) * (TPW * 1024));
  const f32x2* xm = reinterpret_cast<const f32x2*>(smem + LDS_P + (1 - W) * (TPW * 1024) + 4096);
  constexpr int K = 2 * W;
  const f32x2 o = xm[lane];
  const float M = fmaxf(st.m, o[0]);
  float a = __builtin_amdgcn_exp2f(st.m - M), b = __builtin_amdgcn_exp2f(o[0] - M);
  const float inv = 1.0f / fmaf(lsum, a, o[1] * b);
  a *= inv; b *= inv;
#pragma unroll
  for (int j = 0; j < 2; ++j) {
    const f32x4 x0 = xo[(2 * j) * 64 + lane], x1 = xo[(2 * j + 1) * 64 + lane];
    const u32x4 ob = {cvtpk(fmaf(st.acc[0][K + j], a, x0[0] * b), fmaf(st.acc[1][K + j], a, x0[1] * b)),
                      cvtpk(fmaf(st.acc[2][K + j], a, x0[2] * b), fmaf(st.acc[3][K + j], a, x0[3] * b)),
                      cvtpk(fmaf(st.acc[4][K + j], a, x1[0] * b), fmaf(st.acc[5][K + j], a, x1[1] * b)),
                      cvtpk(fmaf(st.acc[6][K + j], a, x1[2] * b), fmaf(st.acc[7][K + j], a, x1[3] * b))};
    *reinterpret_cast<u32x4*>(orow + 8 * (K + j)) = ob;
  }
}

__global__ void __launch_bounds__(NW * 64, 2)
tree_attn_kernel(const unsigned short* __restrict__ q, const unsigned short* __restrict__ kc, const unsigned short* __restrict__ vc,
                 const unsigned short* __restrict__ kt, const unsigned short* __restrict__ vt, const unsigned short* __restrict__ mask,
                 unsigned short* __restrict__ out) {
  extern __shared__ __attribute__((aligned(16))) unsigned char smem[];
  asm volatile("" :: "s"(q), "s"(kc), "s"(vc), "s"(kt), "s"(vt), "s"(mask), "s"(out));
  const int bh = (blockIdx.x & 7) * 32 + (blockIdx.x >> 3);
  const int tid = threadIdx.x, lane = tid & 63, w = __builtin_amdgcn_readfirstlane(tid >> 6);
  const int r = lane & 15, g = lane >> 4;

  const int voff = lane * 16;
  const int koff = (4 * (r & 3) + (r >> 2)) * 256 + g * 16;
  const int qoff = r * 256 + g * 16;
  const u32x4 rk = mkrs(kc, (size_t)bh * HEAD_BYTES, HEAD_BYTES);
  const u32x4 rv = mkrs(vc, (size_t)bh * HEAD_BYTES, HEAD_BYTES);
  const u32x4 rq = mkrs(q, (size_t)bh * (Tn * Dn * 2), Tn * Dn * 2);
  auto toff = [w](int t) -> int { return t < TPW ? (t * NW + w) * 8192 : 0x40000000; };

  u32x4 ring[32];
  u32x4 qf[4], tk[4], tv[4];
  u32x2 mk;
  asm volatile("buffer_load_dwordx4 %0, %4, %5, 0 offen\n\tbuffer_load_dwordx4 %1, %4, %5, 0 offen offset:64\n\t"
               "buffer_load_dwordx4 %2, %4, %5, 0 offen offset:128\n\tbuffer_load_dwordx4 %3, %4, %5, 0 offen offset:192"
               : "=&v"(qf[0]), "=&v"(qf[1]), "=&v"(qf[2]), "=&v"(qf[3]) : "v"(qoff), "s"(rq) : "memory");
  if (w == 1) {
    const u32x4 rtk = mkrs(kt, (size_t)bh * (Tn * Dn * 2), Tn * Dn * 2);
    const u32x4 rtv = mkrs(vt, (size_t)bh * (Tn * Dn * 2), Tn * Dn * 2);
    const u32x4 rm = mkrs(mask, (size_t)(bh / Hn) * (Tn * Tn * 2), Tn * Tn * 2);
    const int tvoff = g * 1024 + r * 16, moff = r * 32 + g * 8;
    asm volatile("buffer_load_dwordx4 %0, %4, %5, 0 offen\n\tbuffer_load_dwordx4 %1, %4, %5, 0 offen offset:64\n\t"
                 "buffer_load_dwordx4 %2, %4, %5, 0 offen offset:128\n\tbuffer_load_dwordx4 %3, %4, %5, 0 offen offset:192"
                 : "=&v"(tk[0]), "=&v"(tk[1]), "=&v"(tk[2]), "=&v"(tk[3]) : "v"(qoff), "s"(rtk) : "memory");
    asm volatile("buffer_load_dwordx4 %0, %4, %5, 0 offen\n\tbuffer_load_dwordx4 %1, %4, %5, 0 offen offset:256\n\t"
                 "buffer_load_dwordx4 %2, %4, %5, 0 offen offset:512\n\tbuffer_load_dwordx4 %3, %4, %5, 0 offen offset:768"
                 : "=&v"(tv[0]), "=&v"(tv[1]), "=&v"(tv[2]), "=&v"(tv[3]) : "v"(tvoff), "s"(rtv) : "memory");
    asm volatile("buffer_load_dwordx2 %0, %1, %2, 0 offen" : "=&v"(mk) : "v"(moff), "s"(rm) : "memory");
  }
  prime<0>(ring, voff, koff, rk, toff(0), toff(1));
#pragma unroll
  for (int s = RUK; s < 32; ++s) asm volatile("" : "=v"(ring[s]));
  SB();

  Lds L;
  L.base = (LAS unsigned char*)smem;
  const int stage = LDS_S + w * 8192;
#pragma unroll
  for (int j = 0; j < 4; ++j) L.wr[j] = stage + g * 256 + ((r ^ (4 * g + j)) * 16);
#pragma unroll
  for (int s = 0; s < 4; ++s) L.rd[s] = stage + (4 * (r & 3) + (r >> 2)) * 256 + (((4 * s + g) ^ r) * 16);
  L.pw = LDS_P + w * (TPW * 1024) + lane * 16;
  L.aw = LDS_A + w * (TPW * 64) + r * 4;

  State st;
  st.m = -INFINITY; st.l = 0.f;
#pragma unroll
  for (int n = 0; n < 8; ++n) st.acc[n] = f32x4{0.f, 0.f, 0.f, 0.f};
  asm volatile("s_waitcnt vmcnt(%c4)" : "+v"(qf[0]), "+v"(qf[1]), "+v"(qf[2]), "+v"(qf[3]) : "i"(RUK) : "memory");
  if (w == 1) {
    asm volatile("" : "+v"(tk[0]), "+v"(tk[1]), "+v"(tk[2]), "+v"(tk[3]), "+v"(tv[0]), "+v"(tv[1]), "+v"(tv[2]), "+v"(tv[3]), "+v"(mk) :: "memory");
    f32x4 s0 = {0.f, 0.f, 0.f, 0.f};
#pragma unroll
    for (int s = 0; s < 4; ++s) s0 = mfma32(tk[s], qf[s], s0);
    const float mv[4] = {__uint_as_float(mk[0] << 16), __uint_as_float(mk[0] & 0xffff0000u), __uint_as_float(mk[1] << 16), __uint_as_float(mk[1] & 0xffff0000u)};
    float x[4];
#pragma unroll
    for (int i = 0; i < 4; ++i) x[i] = fmaf(s0[i], CS, (1.0f - mv[i]) * (-1.0e9f * LOG2E));
    st.m = xrow_max(fmaxf(fmaxf(x[0], x[1]), fmaxf(x[2], x[3])));
    float p[4];
#pragma unroll
    for (int i = 0; i < 4; ++i) p[i] = __builtin_amdgcn_exp2f(x[i] - st.m);
    st.l = (p[0] + p[1]) + (p[2] + p[3]);
    const u32x2 pb = {cvtpk(p[0], p[1]), cvtpk(p[2], p[3])};
#pragma unroll
    for (int wd = 0; wd < 4; ++wd) {
      const u32x2 a0 = {perm_lo(tv[1][wd], tv[0][wd]), perm_lo(tv[3][wd], tv[2][wd])};
      const u32x2 a1 = {perm_hi(tv[1][wd], tv[0][wd]), perm_hi(tv[3][wd], tv[2][wd])};
      st.acc[2 * wd] = mfma16(a0, pb, st.acc[2 * wd]);
      st.acc[2 * wd + 1] = mfma16(a1, pb, st.acc[2 * wd + 1]);
    }
  }
  SB();

#pragma unroll 1
  for (int t0 = 0; t0 < TPW - 4; t0 += 4) {
    Tiles tl;
#pragma unroll
    for (int k = 0; k < NTL; ++k) { tl.rs[k] = rk; tl.so[k] = toff(t0 + k); }
    kbody<0, false>(ring, tl, voff, koff, qf, st, L, t0);
  }
  {
    Tiles tl;
#pragma unroll
    for (int k = 0; k < NTL; ++k) { tl.rs[k] = k < 4 ? rk : rv; tl.so[k] = toff(k < 4 ? TPW - 4 + k : k - 4); }
    kbody<0, true>(ring, tl, voff, koff, qf, st, L, TPW - 4);
  }
  {
    Tiles tl;
#pragma unroll
    for (int k = 0; k < NTL; ++k) { tl.rs[k] = rv; tl.so[k] = toff(k); }
    vbody<0, true>(ring, tl, voff, st, L, 0);
  }
#pragma unroll 1
  for (int t0 = 4; t0 < TPW; t0 += 4) {
    Tiles tl;
#pragma unroll
    for (int k = 0; k < NTL; ++k) { tl.rs[k] = rv; tl.so[k] = toff(t0 + k); }
    vbody<0, false>(ring, tl, voff, st, L, t0);
  }
  drain<0>(ring);

  const float lsum = xrow_sum(st.l);
  unsigned short* orow = out + ((size_t)bh * Tn + r) * Dn + 32 * g;
  if (w == 0) merge_send<0>(st, lsum, smem, lane);
  else        merge_send<1>(st, lsum, smem, lane);
  __syncthreads();
  if (w == 0) merge_recv<0>(st, lsum, smem, lane, orow);
  else        merge_recv<1>(st, lsum, smem, lane, orow);
}

extern "C" void kernel_launch(void* const* d_in, const int* in_sizes, int n_in, void* d_out, int out_size, void* d_ws, size_t ws_size,
                              hipStream_t stream) {
  (void)in_sizes; (void)n_in; (void)out_size; (void)d_ws; (void)ws_size;
  (void)hipFuncSetAttribute((const void*)tree_attn_kernel, hipFuncAttributeMaxDynamicSharedMemorySize, LDS_BYTES);
  tree_attn_kernel<<<dim3(Bn * Hn), dim3(NW * 64), LDS_BYTES, stream>>>(
      (const unsigned short*)d_in[0], (const unsigned short*)d_in[1], (const unsigned short*)d_in[2], (const unsigned short*)d_in[3],
      (const unsigned short*)d_in[4], (const unsigned short*)d_in[5], (unsigned short*)d_out);
}
```
